```python
import torch

def _softmax(x, axis=-1):
    return torch.softmax(x, dim=axis)
def _gelu(x):
    return torch.nn.functional.gelu(x, approximate="tanh")
def _silu(x):
    return torch.nn.functional.silu(x)
def _sigmoid(x):
    return torch.sigmoid(x)
def _layernorm(x, axis=-1, eps=1e-5):
    m = x.mean(dim=axis, keepdim=True)
    v = x.var(dim=axis, keepdim=True, unbiased=False)
    return (x - m) / torch.sqrt(v + eps)
def _scatter_add(tgt, idx, src, axis=0):
    return tgt.clone().index_add_(axis, idx, src)
def _segment_sum(src, seg, n):
    out = torch.zeros((n,) + src.shape[1:], dtype=src.dtype)
    return out.index_add_(0, seg, src)
def _pad(x, pw):
    flat = tuple(v for pair in reversed(pw) for v in pair)
    return torch.nn.functional.pad(x, flat)

kernel_name = 'sequential_preLN__spectral_fnet__geglu_ffn__prompt'

def setup_inputs():

    _w = (lambda a: (lambda t: t.to(torch.bfloat16) if t.is_floating_point() else t)(torch.as_tensor(a)))
    def _A_setup():
        B, S, D = 1, 2048, 640
        ns = torch.arange(S)[:, None] * torch.arange(S)[None, :]
        nd = torch.arange(D)[:, None] * torch.arange(D)[None, :]
        Ws_re = torch.cos(-2.0 * torch.pi * ns / S).to(float)
        Ws_im = torch.sin(-2.0 * torch.pi * ns / S).to(float)
        Wd_re = torch.cos(-2.0 * torch.pi * nd / D).to(float)
        Wd_im = torch.sin(-2.0 * torch.pi * nd / D).to(float)
        return {
            "x": _w((lambda *s: torch.randn(*s, dtype=torch.bfloat16) * 0.05)(B, S, D)),
            "dft_seq_re": _w(Ws_re),
            "dft_seq_im": _w(Ws_im),
            "dft_hid_re": _w(Wd_re),
            "dft_hid_im": _w(Wd_im),
        }
    _A_in = _A_setup()

    def _B_setup():
        B, S, D, F = 1, 2048, 640, 1536
        return {
            "x": _w((lambda *s: torch.randn(*s, dtype=torch.bfloat16) * 0.05)(B, S, D)),
            "w_gate": _w((lambda *s: torch.randn(*s, dtype=torch.bfloat16) * 0.2)(D, F)),
            "w_up": _w((lambda *s: torch.randn(*s, dtype=torch.bfloat16) * 0.2)(D, F)),
            "w_down": _w((lambda *s: torch.randn(*s, dtype=torch.bfloat16) * 0.2)(F, D)),
        }
    _B_in = _B_setup()

    return {
        "A_x": _A_in["x"],
        "A_dft_seq_re": _A_in["dft_seq_re"],
        "A_dft_seq_im": _A_in["dft_seq_im"],
        "A_dft_hid_re": _A_in["dft_hid_re"],
        "A_dft_hid_im": _A_in["dft_hid_im"],
        "B_x": _B_in["x"],
        "B_w_gate": _B_in["w_gate"],
        "B_w_up": _B_in["w_up"],
        "B_w_down": _B_in["w_down"],
    }

def reference(A_x, A_dft_seq_re, A_dft_seq_im, A_dft_hid_re, A_dft_hid_im, B_x, B_w_gate, B_w_up, B_w_down):
    def _A_ref(x, dft_seq_re, dft_seq_im, dft_hid_re, dft_hid_im):
        a = torch.einsum("bsd,ed->bse", x, dft_hid_re)
        b = torch.einsum("bsd,ed->bse", x, dft_hid_im)
        y = torch.einsum("ts,bse->bte", dft_seq_re, a) - torch.einsum("ts,bse->bte", dft_seq_im, b)
        return y

    def _B_ref(x, w_gate, w_up, w_down):
        return (_gelu(x @ w_gate) * (x @ w_up)) @ w_down

    x = A_x
    h = x + _A_ref(_layernorm(x, axis=-1), A_dft_seq_re, A_dft_seq_im, A_dft_hid_re, A_dft_hid_im)
    h2 = h + _B_ref(_layernorm(h, axis=-1), B_w_gate, B_w_up, B_w_down)
    return h2
```

```cpp
#include <hip/hip_runtime.h>
#include <hip/hip_bf16.h>
#include <stdint.h>
#include <utility>

typedef unsigned short u16;
typedef unsigned int u32;
using bf16x8 = __attribute__((ext_vector_type(8))) short;
using f32x4 = __attribute__((ext_vector_type(4))) float;
using f32x2 = __attribute__((ext_vector_type(2))) float;
using u32x4 = __attribute__((ext_vector_type(4))) unsigned;
using u32x2 = __attribute__((ext_vector_type(2))) unsigned;
using bf16x2_t = __attribute__((ext_vector_type(2))) __bf16;

constexpr int S_ = 2048, D_ = 640, F_ = 1536;

__device__ __forceinline__ float bflo(u32 v) { return __uint_as_float(v << 16); }
__device__ __forceinline__ float bfhi(u32 v) { return __uint_as_float(v & 0xffff0000u); }
__device__ __forceinline__ u32 pack2(float a, float b) {
  f32x2 v = {a, b};
  bf16x2_t r = __builtin_convertvector(v, bf16x2_t);
  return __builtin_bit_cast(u32, r);
}
__device__ __forceinline__ float rbf(float f) { return bflo(pack2(f, f)); }

__device__ __forceinline__ void glds16(u32 voff, const void* sbase, u32 ldsaddr) {
  asm volatile("s_mov_b32 m0, %2\n\ts_nop 0\n\tglobal_load_lds_dwordx4 %0, %1" ::"v"(voff), "s"(sbase), "s"(ldsaddr)
               : "memory");
}
__device__ __forceinline__ void touch(u32& sink, u32 voff, const void* sbase) {
  asm volatile("global_load_dword %0, %1, %2" : "+v"(sink) : "v"(voff), "s"(sbase) : "memory");
}
__device__ __forceinline__ void st16_dev(u32 voff, void* sbase, u32x4 v) {
  asm volatile("global_store_dwordx4 %0, %1, %2 sc1\n\ts_nop 1" ::"v"(voff), "v"(v), "s"(sbase) : "memory");
}
__device__ __forceinline__ void st8_dev(u32 voff, void* sbase, u32x2 v) {
  asm volatile("global_store_dwordx2 %0, %1, %2 sc1\n\ts_nop 1" ::"v"(voff), "v"(v), "s"(sbase) : "memory");
}
__device__ __forceinline__ void ld16_dev(u32x4& r, u32 voff, const void* sbase) {
  asm volatile("global_load_dwordx4 %0, %1, %2 sc1" : "=v"(r) : "v"(voff), "s"(sbase) : "memory");
}
__device__ __forceinline__ void ld8_dev(u32x2& r, u32 voff, const void* sbase) {
  asm volatile("global_load_dwordx2 %0, %1, %2 sc1" : "=v"(r) : "v"(voff), "s"(sbase) : "memory");
}
#define CODE_ADDR(p, sym)                                                                           \
  do {                                                                                              \
    const char* pc_;                                                                                \
    int off_;                                                                                       \
    asm volatile("s_getpc_b64 %0\n\ts_mov_b32 %1, " sym "@rel32@lo+4" : "=s"(pc_), "=s"(off_)); \
    p = pc_ + off_;                                                                                 \
  } while (0)
#define SCHED() __builtin_amdgcn_sched_barrier(0)
template <int N> __device__ __forceinline__ void wait_vm() {
  static_assert(N >= 0 && N <= 63, "vmcnt range");
  asm volatile("s_waitcnt vmcnt(%0)" ::"n"(N) : "memory");
}
__device__ __forceinline__ void bar() {
  asm volatile("" ::: "memory");
  __builtin_amdgcn_s_waitcnt(0xC07F);
  __builtin_amdgcn_s_barrier();
  asm volatile("" ::: "memory");
}
template <int CTRL> __device__ __forceinline__ float dpp_mov(float v) {
  return __int_as_float(__builtin_amdgcn_update_dpp(0, __float_as_int(v), CTRL, 0xf, 0xf, false));
}
__device__ __forceinline__ float sum8(float v) {
  v += dpp_mov<0xB1>(v);
  v += dpp_mov<0x4E>(v);
  v += dpp_mov<0x141>(v);
  return v;
}
template <class F, int... I> __device__ __forceinline__ void static_for_impl(F&& f, std::integer_sequence<int, I...>) {
  (f(std::integral_constant<int, I>{}), ...);
}
template <int N, class F> __device__ __forceinline__ void static_for(F&& f) {
  static_for_impl(f, std::make_integer_sequence<int, N>{});
}
__device__ __forceinline__ f32x2 ln_stats(float sum, float sumsq) {
  float mean = sum * (1.f / D_);
  float var = fmaxf(sumsq * (1.f / D_) - mean * mean, 0.f);
  float sd = rbf(__builtin_amdgcn_sqrtf(rbf(rbf(var) + 1e-5f)));
  return f32x2{rbf(mean), __builtin_amdgcn_rcpf(sd)};
}

__device__ __forceinline__ u32 toff(int r, int k, int K) {
  return (u32)((((r >> 4) * (K >> 5) + (k >> 5)) << 10) + ((r & 15) << 6) + ((k & 31) << 1));
}

template <int MODE> struct Cfg;
template <> struct Cfg<1> { static constexpr int RP = 128, RQ = 80, WP = 4, WQ = 1, WK = 1, NKT = 10, NST = 4, LDP = 640, LDQ = 640, TT = 3; static constexpr bool TP = false, TQ = false; };
template <> struct Cfg<2> { static constexpr int RP = 160, RQ = 128, WP = 2, WQ = 2, WK = 1, NKT = 16, NST = 4, LDP = 2048, LDQ = 2048, TT = 8; static constexpr bool TP = true, TQ = false; };
template <> struct Cfg<3> { static constexpr int RP = 192, RQ = 128, WP = 2, WQ = 2, WK = 1, NKT = 10, NST = 4, LDP = 640, LDQ = 640, TT = 15; static constexpr bool TP = true, TQ = true; };
template <> struct Cfg<4> { static constexpr int RP = 80, RQ = 64, WP = 1, WQ = 2, WK = 2, NKT = 24, NST = 5, LDP = 1536, LDQ = 1536, TT = 0; static constexpr bool TP = true, TQ = true; };

constexpr u32 WS_BYTES = (u32)((3 * S_ * D_ + 3 * F_ * D_ + S_ * F_) * 2 + S_ * 64 + 10 * 2 * F_ * 4 + 65536 + 256 * 16 * 2560);
struct Args {
  const u16* x;
  const u16* Cr;
  const u16* Ci;
  const u16* Wr;
  const u16* Wi;
  const u16* wg;
  const u16* wu;
  const u16* wd;
  u16* ab;
  char* xbuf;
  u32* flags;
  u16* h;
  float* hstat;
  u16* wguP;
  u16* wdP;
  float* colp;
  u16* act;
  u16* out;
};

template <int MODE, int GP, int GQ>
__device__ __forceinline__ void loader(const Args& a, const char* Pb, const char* Qb, const char* Tb, int aux, char* smem,
                                       u32 lds0, int lw, int lane, const u32* voff) {
  using C = Cfg<MODE>;
  constexpr int NGP = C::RP / 8;
  constexpr int G = GP + GQ;
  constexpr int SB = (C::RP + C::RQ) * 128;
  constexpr int GPM = (NGP + 3) / 4;
  const u32 ldsw = lds0 + lw * 1024;
  u32 ldsb = ldsw + 2 * SB;
  const char *pP = Pb + 2 * (C::TP ? 2048 : 128), *pQ = Qb + 2 * (C::TQ ? 2048 : 128);
  auto issue = [&]() {
#pragma unroll
    for (int i = 0; i < GP; ++i) glds16(voff[i], pP, ldsb + i * 4096);
#pragma unroll
    for (int i = 0; i < GQ; ++i) glds16(voff[GPM + i], pQ, ldsb + NGP * 1024 + i * 4096);
    pP += C::TP ? 2048 : 128;
    pQ += C::TQ ? 2048 : 128;
    ldsb = (ldsb + SB == ldsw + C::NST * SB) ? ldsw : ldsb + SB;
  };
  constexpr int R = C::NST - 3, DD = C::NST - 2;
  static_assert(C::NKT >= 3 + 2 * R && C::NST >= 3, "ramp");
  wait_vm<G>();
  bar();
  issue();
  static_for<R>([&](auto rc) {
    constexpr int r = decltype(rc)::value;
    wait_vm<(r + 1) * G>();
    bar();
    issue();
    issue();
  });
  const int bid = blockIdx.x;
  const bool has = bid < 240 && lw < 3;
  const int tw = lw < 3 ? lw : 0, tr = bid < 240 ? bid : 0;
  const int ldsrc = tw < 2 ? F_ : D_, kdim = tw < 2 ? D_ : F_;
  const int tkb = tw < 2 ? tr / 24 : tr / 10, tnb = tw < 2 ? tr % 24 : tr % 10;
#pragma unroll 1
  for (int s = R; s < C::NKT - C::NST; ++s) {
    wait_vm<DD * G>();
    bar();
    issue();
  }
  u32 sink = 0;
  u32x4 pin[8];
  float cv[10];
  f32x4 hv[4];
  if (MODE == 1) {
    const u32 to = (u32)((lane >> 3) * 4096 + (lane & 7) * 128);
    {
      const int jj = bid >> 3;
      constexpr u32 WSB = WS_BYTES, WB = (u32)D_ * F_ * 2, OB = (u32)S_ * D_ * 2;
      const char* base;
      u32 off, lim;
      if (lw == 1) { base = (const char*)(jj & 1 ? a.wu : a.wg); off = (u32)((jj >> 1) * 64 + lane); lim = WB; }
      else if (lw == 2) { base = jj & 1 ? (const char*)a.out : (const char*)a.wd; off = (u32)((jj >> 1) * 64 + lane); lim = jj & 1 ? OB : WB; }
      else { base = (const char*)a.ab; off = (u32)(((lw ? 32 : 0) + jj) * 16 + (lane & 15)); lim = WSB; }
      off *= 65536u;
      touch(sink, off < lim ? off : 0u, base);
    }
    touch(sink, to, Tb + (size_t)(lw * 8) * 4096);
    touch(sink, to + 1024, Tb + (size_t)(lw * 8) * 4096);
  }
  if (MODE == 2) {
    const u16 *w0 = a.wg, *w1 = a.wu, *w2 = a.wd;
    asm volatile("" : "+s"(w0), "+s"(w1), "+s"(w2));
    const u16* src = has ? (tw == 0 ? w0 : tw == 1 ? w1 : w2) + (size_t)(tkb * 64) * ldsrc + tnb * 64 : (const u16*)Qb;
    const u32 vo = has ? (u32)(((lane & 7) * 8 * ldsrc + (lane >> 3) * 8) * 2) : 0u;
    const int sstep = has ? ldsrc : 0;
#pragma unroll
    for (int i = 0; i < 8; ++i)
      asm volatile("global_load_dwordx4 %0, %1, %2 nt" : "=v"(pin[i]) : "v"(vo), "s"(src + i * sstep) : "memory");
  }
  if (MODE == 3) {
    const u32 co = (u32)(((lw < 3 ? lw : 0) * 64 + lane) * 4);
#pragma unroll
    for (int kb = 0; kb < 10; ++kb)
      asm volatile("global_load_dword %0, %1, %2" : "=v"(cv[kb]) : "v"(co), "s"(Tb + (size_t)kb * (2 * F_) * 4) : "memory");
    const u32 ho = (u32)(((lw & 1) * 64 + lane) * 64);
#pragma unroll
    for (int k = 0; k < 4; ++k)
      asm volatile("global_load_dwordx4 %0, %1, %2" : "=v"(hv[k]) : "v"(ho + k * 16), "s"(a.hstat + (size_t)aux * 16) : "memory");
    {
      const char* wb = (const char*)a.wdP + (size_t)(bid & 1) * 983040 + (size_t)((bid >> 3) * 240 + lw * 60) * 128;
      touch(sink, (u32)(lane < 60 ? lane : 0) * 128, wb);
    }
  }
  static_for<C::NST - 1>([&](auto tc) {
    constexpr int t = decltype(tc)::value;
    wait_vm<(DD - t) * G + C::TT>();
    bar();
  });
  float csum = 0.f;
  f32x2 tstat = {0.f, 0.f};
  if (MODE == 3) {
    asm volatile("s_waitcnt vmcnt(1)"
                 : "+v"(cv[0]), "+v"(cv[1]), "+v"(cv[2]), "+v"(cv[3]), "+v"(cv[4]), "+v"(cv[5]), "+v"(cv[6]), "+v"(cv[7]), "+v"(cv[8]),
                   "+v"(cv[9]), "+v"(hv[0]), "+v"(hv[1]), "+v"(hv[2]), "+v"(hv[3])::"memory");
#pragma unroll
    for (int kb = 0; kb < 10; ++kb) csum += cv[kb];
    f32x4 t4 = (hv[0] + hv[1]) + (hv[2] + hv[3]);
    tstat = ln_stats(t4[0] + t4[2], t4[1] + t4[3]);
  }
  bar();
  if (MODE == 3) {
    if (lw < 3) ((float*)smem)[lw * 64 + lane] = csum;
    if (lw >= 2) *(f32x2*)(smem + 1024 + ((lw & 1) * 64 + lane) * 8) = tstat;
  }
  if (MODE != 2) bar();
  if (MODE == 1 || MODE == 3) asm volatile("s_waitcnt vmcnt(0)" : "+v"(sink)::"memory");
  if (MODE == 2) {
    asm volatile("s_waitcnt vmcnt(0)"
                 : "+v"(pin[0]), "+v"(pin[1]), "+v"(pin[2]), "+v"(pin[3]), "+v"(pin[4]), "+v"(pin[5]), "+v"(pin[6]), "+v"(pin[7])::"memory");
    if (has) {
      const int kcol = tkb * 64 + (lane & 7) * 8;
      const int ncol = tnb * 64 + (lane >> 3) * 8;
      int row0, rstep;
      if (tw < 2) {
        int fb = ncol / 96, fl = ncol % 96;
        row0 = fb * 192 + (fl < 64 ? (fl >> 5) * 96 + tw * 16 + ((fl & 31) >> 3) * 4 : ((fl - 64) >> 4) * 96 + (4 + tw) * 16 + (fl & 15));
        rstep = fl < 64 ? 32 : 4;
      } else {
        int eb = ncol / 80, el = ncol % 80;
        row0 = eb * 80 + (el < 64 ? (el >> 5) * 32 + ((el & 31) >> 3) * 4 : el);
        rstep = el < 64 ? 16 : 4;
      }
      char* dst = (char*)(tw < 2 ? a.wguP : a.wdP);
      const u32 o0 = toff(row0, kcol, kdim), o1 = toff(row0 + rstep, kcol, kdim);
      const bf16x2_t ones = __builtin_bit_cast(bf16x2_t, 0x3f803f80u);
      float csel = 0.f;
#pragma unroll
      for (int jj = 0; jj < 8; ++jj) {
        u32x4 o;
        float cs = 0.f;
#pragma unroll
        for (int m = 0; m < 4; ++m) {
          u32 t = __builtin_amdgcn_perm(pin[2 * m + 1][jj >> 1], pin[2 * m][jj >> 1], (jj & 1) ? 0x07060302u : 0x05040100u);
          o[m] = t;
          cs = __builtin_amdgcn_fdot2_f32_bf16(__builtin_bit_cast(bf16x2_t, t), ones, cs, false);
        }
        *(u32x4*)(dst + ((jj >> 2) ? o1 : o0) + (jj & 3) * 64) = o;
        cs = sum8(cs);
        csel = (lane & 7) == jj ? cs : csel;
      }
      if (tw < 2) a.colp[tkb * (2 * F_) + row0 + ((lane & 7) >> 2) * rstep + (lane & 3)] = csel;
    }
  }
}
static_assert(sizeof(Args) == 144, "kernarg layout");
using s32x16 = __attribute__((ext_vector_type(16))) unsigned;
template <int MODE> __global__ __launch_bounds__(512, 2) void gemm_kernel(Args karg) {
  using C = Cfg<MODE>;
  s32x16 ka0, ka1;
  u32x4 ka2;
  {
    const void* kp = (const void*)__builtin_amdgcn_kernarg_segment_ptr();
    asm volatile("s_load_dwordx16 %0, %3, 0x0\n\ts_load_dwordx16 %1, %3, 0x40\n\ts_load_dwordx4 %2, %3, 0x80"
                 : "=&s"(ka0), "=&s"(ka1), "=&s"(ka2)
                 : "s"(kp));
  }
  constexpr int SB = (C::RP + C::RQ) * 128;
  constexpr int PT = C::RP / C::WP / 16, QT = C::RQ / C::WQ / 16;
  constexpr int KS = 2 / C::WK;
  constexpr int LDS_BYTES = C::NST * SB + (MODE == 1 ? 256 : 0);
  __shared__ __attribute__((aligned(1024))) char smem[LDS_BYTES];
  const int lane = threadIdx.x & 63;
  const int wave = __builtin_amdgcn_readfirstlane(threadIdx.x >> 6);
  const int bid = blockIdx.x;
  const int xcd = bid & 7, j = bid >> 3;
  u32 csink = 0;
  if (MODE == 1 && j == 0 && wave < 3) {
    const char *c1, *c4;
    CODE_ADDR(c1, "_Z11gemm_kernelILi1EEv4Args");
    CODE_ADDR(c4, "_Z11gemm_kernelILi4EEv4Args");
    const long span = c4 - c1;
    const u32 lim = (span > 0 && span < 65536) ? (u32)span + 3840u : 4096u;
    const u32 o = (u32)threadIdx.x * 128;
    touch(csink, o < lim ? o : 0u, c1);
  }
  if (MODE > 1 && j == 0 && wave == 0) {
    const char* c;
    if (MODE == 2) CODE_ADDR(c, "_Z11gemm_kernelILi2EEv4Args");
    else if (MODE == 3) CODE_ADDR(c, "_Z11gemm_kernelILi3EEv4Args");
    else CODE_ADDR(c, "_Z11gemm_kernelILi4EEv4Args");
    const u32 o = (u32)lane * 128;
    touch(csink, o < (MODE == 2 ? 7296u : MODE == 3 ? 6656u : 3840u) ? o : 0u, c);
  }
  constexpr int NGP_ = C::RP / 8, NGQ_ = C::RQ / 8, GPM = (NGP_ + 3) / 4, GQM = (NGQ_ + 3) / 4;
  u32 voff[GPM + GQM];
  {
    const int lw = wave & 3, cc = (lane & 7) ^ (lane >> 3);
    const int ax = MODE == 1 ? (j & 1) : (xcd & 3);
#pragma unroll
    for (int i = 0; i < GPM; ++i) {
      int row = (lw + 4 * i) * 8 + (lane >> 3);
      if (C::TP) voff[i] = toff(row, cc * 8, C::LDP);
      else voff[i] = (u32)(((row >> 5) * 32 + ((row & 15) >> 2) * 8 + ((row >> 4) & 1) * 4 + (row & 3)) * C::LDP * 2 + cc * 16);
    }
#pragma unroll
    for (int i = 0; i < GQM; ++i) {
      int q = (lw + 4 * i) * 8 + (lane >> 3);
      if (C::TQ) voff[GPM + i] = toff(q, cc * 8, C::LDQ);
      else if (MODE == 1) {
        const int n = (lw + 4 * i) >> 1, rr = q & 15;
        const int ub = n < 4 ? ax * 64 + (n >> 1) * 32 + (n & 1) * 4 : 128 + ax * 16;
        const int lp = n < 4 ? (rr >> 2) * 8 + (rr & 3) : rr;
        voff[GPM + i] = (u32)((ub + lp) * C::LDQ * 2 + cc * 16);
      }
      else voff[GPM + i] = (u32)(((q & ~63) + ((((q >> 4) & 3) + ax) & 3) * 16 + (q & 15)) * C::LDQ * 2 + cc * 16);
    }
#pragma unroll
    for (int i = 0; i < GPM + GQM; ++i) asm volatile("" : "+v"(voff[i]));
  }
  int p0, q0, var = 0, aux = 0, eb = 0;
  u32 offP, offQ;
  if (MODE == 1) {
    int sb = (xcd >> 1) * 4 + (j >> 3);
    eb = j & 7;
    var = xcd & 1;
    p0 = sb * 128; q0 = eb * 80; aux = eb & 1;
    offP = (u32)p0 * C::LDP * 2;
    offQ = (u32)(eb >> 1) * 160 * C::LDQ * 2;
  } else if (MODE == 2) {
    int ri = xcd & 1, kh = (xcd >> 1) & 1;
    int tb = (xcd >> 2) * 8 + (j >> 2);
    eb = j & 3;
    var = xcd & 3; aux = var;
    p0 = eb * 160; q0 = tb * 128;
    offP = (u32)ri * S_ * D_ * 2 + toff(p0, kh * 1024, C::LDP);
    offQ = (u32)(q0 * C::LDQ + kh * 1024) * 2;
  } else if (MODE == 3) {
    int tb = (xcd >> 2) * 8 + (j >> 2), fb = (xcd & 3) * 4 + (j & 3);
    p0 = fb * 96; q0 = tb * 128; aux = q0;
    offP = toff(fb * 192, 0, C::LDP);
    offQ = toff(q0, 0, C::LDQ);
  } else {
    int tb = (xcd >> 1) * 8 + (j >> 2);
    eb = (xcd & 1) * 4 + (j & 3);
    p0 = eb * 80; q0 = tb * 64;
    offP = toff(p0, 0, C::LDP);
    offQ = toff(q0, 0, C::LDQ);
  }
  asm volatile("" : "+s"(offP), "+s"(offQ));
  asm volatile("s_waitcnt lgkmcnt(0)" : "+s"(ka0), "+s"(ka1), "+s"(ka2));
  Args a;
  {
    typedef __attribute__((address_space(1))) char* gptr;
    auto P0 = [&](int i) { return (char*)(gptr)((unsigned long long)ka0[2 * i] | ((unsigned long long)ka0[2 * i + 1] << 32)); };
    auto P1 = [&](int i) { return (char*)(gptr)((unsigned long long)ka1[2 * i] | ((unsigned long long)ka1[2 * i + 1] << 32)); };
    auto P2 = [&](int i) { return (char*)(gptr)((unsigned long long)ka2[2 * i] | ((unsigned long long)ka2[2 * i + 1] << 32)); };
    a.x = (const u16*)P0(0); a.Cr = (const u16*)P0(1); a.Ci = (const u16*)P0(2); a.Wr = (const u16*)P0(3);
    a.Wi = (const u16*)P0(4); a.wg = (const u16*)P0(5); a.wu = (const u16*)P0(6); a.wd = (const u16*)P0(7);
    a.ab = (u16*)P1(0); a.xbuf = (char*)P1(1); a.flags = (u32*)P1(2); a.h = (u16*)P1(3);
    a.hstat = (float*)P1(4); a.wguP = (u16*)P1(5); a.wdP = (u16*)P1(6); a.colp = (float*)P1(7);
    a.act = (u16*)P2(0); a.out = (u16*)P2(1);
  }
  const char *Pb, *Qb, *Tb = nullptr;
  if (MODE == 1) {
    Pb = (const char*)a.x + offP;
    Qb = (const char*)(var ? a.Wi : a.Wr) + offQ;
    Tb = (const char*)((xcd & 1) ? a.Ci : a.Cr) + (size_t)((xcd >> 2) * 1024 + j * 32) * 4096 + ((xcd >> 1) & 1) * 2048;
  } else if (MODE == 2) {
    Pb = (const char*)a.ab + offP;
    Qb = (const char*)((xcd & 1) ? a.Ci : a.Cr) + offQ;
  } else if (MODE == 3) {
    Pb = (const char*)a.wguP + offP;
    Qb = (const char*)a.h + offQ;
    Tb = (const char*)(a.colp + p0 * 2);
  } else {
    Pb = (const char*)a.wdP + offP;
    Qb = (const char*)a.act + offQ;
  }
  const u32 lds0 = (u32)(size_t)(__attribute__((address_space(3))) char*)smem;
  if (wave >= 4) {
    const u32 lb = lds0 + (wave - 4) * 1024;
#pragma unroll
    for (int st = 0; st < 2; ++st) {
      const char *pP = Pb + st * (C::TP ? 2048 : 128), *pQ = Qb + st * (C::TQ ? 2048 : 128);
#pragma unroll
      for (int i = 0; i < NGP_ / 4; ++i) glds16(voff[i], pP, lb + st * SB + i * 4096);
#pragma unroll
      for (int i = 0; i < NGQ_ / 4; ++i) glds16(voff[GPM + i], pQ, lb + st * SB + NGP_ * 1024 + i * 4096);
      if ((NGP_ % 4 || NGQ_ % 4) && wave < 6) {
        if (NGP_ % 4) glds16(voff[GPM - 1], pP, lb + st * SB + (GPM - 1) * 4096);
        if (NGQ_ % 4) glds16(voff[GPM + GQM - 1], pQ, lb + st * SB + NGP_ * 1024 + (GQM - 1) * 4096);
      }
    }
  }
  asm volatile("" ::: "memory");
  if (__builtin_expect(__builtin_amdgcn_readfirstlane(threadIdx.x) >= 256, 1)) {
    constexpr int NGP = C::RP / 8, NGQ = C::RQ / 8;
    const int lw = wave - 4;
    if (NGP % 4 == 0 && NGQ % 4 == 0) loader<MODE, NGP / 4, NGQ / 4>(a, Pb, Qb, Tb, aux, smem, lds0, lw, lane, voff);
    else if (lw < 2) loader<MODE, (NGP + 3) / 4, (NGQ + 3) / 4>(a, Pb, Qb, Tb, aux, smem, lds0, lw, lane, voff);
    else loader<MODE, NGP / 4, NGQ / 4>(a, Pb, Qb, Tb, aux, smem, lds0, lw, lane, voff);
    return;
  }
  const int wp = wave % C::WP, wq = (wave / C::WP) % C::WQ, wk = wave / (C::WP * C::WQ);
  const int fr = lane & 15, fq = lane >> 4;
  if (MODE == 1 && lane < 4) a.flags[(bid * 4 + wave) * 16 + lane] = 0u;
  u32x4 h0, h1;
  u32x2 h2;
  f32x4 acc[PT][QT];
#pragma unroll
  for (int i = 0; i < PT; ++i)
#pragma unroll
    for (int n = 0; n < QT; ++n) acc[i][n] = f32x4{0.f, 0.f, 0.f, 0.f};
  const int sw0 = ((0 * 4 + fq) ^ (fr & 7)) * 16, sw1 = ((1 * 4 + fq) ^ (fr & 7)) * 16;
  const int prow = (wp * PT * 16 + fr) * 128, qrow = (C::RP + wq * QT * 16 + fr) * 128;
  constexpr int QX = QT + (MODE == 1 ? 1 : 0);
  bf16x8 fp0[PT], fq0[QX], fp1[PT], fq1[QX];
  auto rd = [&](bf16x8(&fp)[PT], bf16x8(&fqq)[QX], int buf, int sw) {
    const char* b = smem + buf * SB;
#pragma unroll
    for (int n = 0; n < QT; ++n) fqq[n] = *(const bf16x8*)(b + qrow + sw + n * 2048);
    if (MODE == 1) fqq[QX - 1] = *(const bf16x8*)(b + qrow + sw + wave * 2048);
#pragma unroll
    for (int i = 0; i < PT; ++i) fp[i] = *(const bf16x8*)(b + prow + sw + i * 2048);
  };
  f32x4 asum[PT], asq[PT];
#pragma unroll
  for (int i = 0; i < PT; ++i) { asum[i] = f32x4{0.f, 0.f, 0.f, 0.f}; asq[i] = f32x4{0.f, 0.f, 0.f, 0.f}; }
  f32x4 aw = {0.f, 0.f, 0.f, 0.f}, aw4 = aw;
  auto mm = [&](bf16x8(&fp)[PT], bf16x8(&fqq)[QX]) {
    const bf16x8 ones = {0x3f80, 0x3f80, 0x3f80, 0x3f80, 0x3f80, 0x3f80, 0x3f80, 0x3f80};
#pragma unroll
    for (int i = 0; i < PT; ++i) {
#pragma unroll
      for (int n = 0; n < QT; ++n) acc[i][n] = __builtin_amdgcn_mfma_f32_16x16x32_bf16(fp[i], fqq[n], acc[i][n], 0, 0, 0);
      if (MODE == 1) {
        asum[i] = __builtin_amdgcn_mfma_f32_16x16x32_bf16(fp[i], ones, asum[i], 0, 0, 0);
        asq[i] = __builtin_amdgcn_mfma_f32_16x16x32_bf16(fp[i], fp[i], asq[i], 0, 0, 0);
      }
    }
    if (MODE == 1) {
      aw4 = __builtin_amdgcn_mfma_f32_16x16x32_bf16(ones, fqq[QT - 1], aw4, 0, 0, 0);
      aw = __builtin_amdgcn_mfma_f32_16x16x32_bf16(ones, fqq[QX - 1], aw, 0, 0, 0);
    }
  };
  auto ilv = [&]() {
    constexpr int NR = PT + QX, NM = PT * QT + (MODE == 1 ? 2 * PT + 2 : 0);
    constexpr int TAIL = NM >= 20 ? 5 : 3;
    constexpr int SP = NM - TAIL;
#pragma unroll
    for (int i = 0; i < NR; ++i) {
      int m = (i + 1) * SP / NR - i * SP / NR;
      if (m == 1) __builtin_amdgcn_sched_group_barrier(0x008, 1, 0);
      if (m == 2) __builtin_amdgcn_sched_group_barrier(0x008, 2, 0);
      if (m == 3) __builtin_amdgcn_sched_group_barrier(0x008, 3, 0);
      __builtin_amdgcn_sched_group_barrier(0x100, 1, 0);
    }
    __builtin_amdgcn_sched_group_barrier(0x008, TAIL, 0);
    SCHED();
  };
  const int swk = wk ? sw1 : sw0;
  bar();
  if (MODE == 4) {
    int t = q0 + wq * 32 + wk * 16 + fr;
    const char* hb = (const char*)a.h;
    h0 = *(const u32x4*)(hb + toff(t, p0 + fq * 8, D_));
    h1 = *(const u32x4*)(hb + toff(t, p0 + 32 + fq * 8, D_));
    h2 = *(const u32x2*)(hb + toff(t, p0 + 64 + fq * 4, D_));
  }
  if (KS == 1) {
    rd(fp0, fq0, 0, swk);
    SCHED();
    int buf = 0;
#pragma unroll 1
    for (int s = 0; s < C::NKT; s += 2) {
      int b1 = buf + 1; if (b1 == C::NST) b1 = 0;
      bar();
      rd(fp1, fq1, b1, swk);
      mm(fp0, fq0);
      ilv();
      int b2 = b1 + 1; if (b2 == C::NST) b2 = 0;
      bar();
      rd(fp0, fq0, b2, swk);
      mm(fp1, fq1);
      ilv();
      buf = b2;
    }
  } else {
    rd(fp0, fq0, 0, sw0);
    SCHED();
    int buf = 0;
#pragma unroll 1
    for (int s = 0; s < C::NKT; ++s) {
      rd(fp1, fq1, buf, sw1);
      mm(fp0, fq0);
      ilv();
      int b1 = buf + 1; if (b1 == C::NST) b1 = 0;
      bar();
      rd(fp0, fq0, b1, sw0);
      mm(fp1, fq1);
      ilv();
      buf = b1;
    }
  }
  auto res = [&](u32 h, float x, float y) { return pack2(bflo(h) + x, bfhi(h) + y); };
  if (MODE == 1) {
    float* st = (float*)(smem + C::NST * SB);
    if (fq == 0) st[wave * 16 + fr] = aw[0];
    const float sgn = var ? -1.f : 1.f;
    float mrow[2][4], rrow[2][4];
#pragma unroll
    for (int i = 0; i < 2; ++i)
#pragma unroll
      for (int k = 0; k < 4; ++k) {
        f32x2 ms = ln_stats(asum[i][k], __shfl(asq[i][k], fq * 20 + k));
        mrow[i][k] = ms[0];
        rrow[i][k] = ms[1] * sgn;
      }
    char* outp = (char*)(a.ab + (size_t)var * S_ * D_);
    bar();
#pragma unroll
    for (int n = 0; n < QT; ++n) {
      float wsum = n < 4 ? st[n * 16 + fr] : aw4[0];
      u32x4 o;
#pragma unroll
      for (int i = 0; i < 2; ++i) {
        f32x4 v = acc[i][n];
        float t0 = (v[0] - mrow[i][0] * wsum) * rrow[i][0], t1 = (v[1] - mrow[i][1] * wsum) * rrow[i][1];
        float t2 = (v[2] - mrow[i][2] * wsum) * rrow[i][2], t3 = (v[3] - mrow[i][3] * wsum) * rrow[i][3];
        o[i * 2] = pack2(t0, t1); o[i * 2 + 1] = pack2(t2, t3);
      }
      *(u32x4*)(outp + toff(q0 + n * 16 + fr, p0 + wp * 32 + fq * 8, S_)) = o;
    }
  } else if (MODE == 2) {
#pragma unroll
    for (int n = 1; n < 4; ++n) {
      char* slot = a.xbuf + (size_t)((((bid & ~3) | ((var + n) & 3)) * 4 + var) * 4 + wave) * 2560;
      u32x4 o0 = {pack2(acc[0][n][0], acc[0][n][1]), pack2(acc[0][n][2], acc[0][n][3]), pack2(acc[1][n][0], acc[1][n][1]), pack2(acc[1][n][2], acc[1][n][3])};
      u32x4 o1 = {pack2(acc[2][n][0], acc[2][n][1]), pack2(acc[2][n][2], acc[2][n][3]), pack2(acc[3][n][0], acc[3][n][1]), pack2(acc[3][n][2], acc[3][n][3])};
      u32x2 o2 = {pack2(acc[4][n][0], acc[4][n][1]), pack2(acc[4][n][2], acc[4][n][3])};
      st16_dev(lane * 16, slot, o0);
      st16_dev(1024 + lane * 16, slot, o1);
      st8_dev(2048 + lane * 8, slot, o2);
    }
    {
      const u16* xr = a.x + (size_t)(q0 + wq * 64 + var * 16 + fr) * D_ + p0;
      h0 = *(const u32x4*)(xr + wp * 64 + fq * 8);
      h1 = *(const u32x4*)(xr + wp * 64 + 32 + fq * 8);
      h2 = *(const u32x2*)(xr + 128 + wp * 16 + fq * 4);
    }
    asm volatile("s_waitcnt vmcnt(0)" ::: "memory");
    if (lane >= 1 && lane < 4)
      __hip_atomic_store(a.flags + (((bid & ~3) | ((var + lane) & 3)) * 4 + wave) * 16 + lane, 1u, __ATOMIC_RELAXED, __HIP_MEMORY_SCOPE_AGENT);
    {
      u32* f = a.flags + (bid * 4 + wave) * 16 + 1 + (lane < 3 ? lane : 0);
      for (int spins = 0; spins < (1 << 21); ++spins) {
        u32 v = __hip_atomic_load(f, __ATOMIC_RELAXED, __HIP_MEMORY_SCOPE_AGENT);
        if (__all(v == 1u)) break;
        __builtin_amdgcn_s_sleep(1);
      }
    }
    u32x4 r0[3], r1[3];
    u32x2 r2[3];
#pragma unroll
    for (int n = 1; n < 4; ++n) {
      const char* slot = a.xbuf + (size_t)((bid * 4 + ((var + n) & 3)) * 4 + wave) * 2560;
      ld16_dev(r0[n - 1], lane * 16, slot);
      ld16_dev(r1[n - 1], 1024 + lane * 16, slot);
      ld8_dev(r2[n - 1], 2048 + lane * 8, slot);
    }
    asm volatile("s_waitcnt vmcnt(0)"
                 : "+v"(r0[0]), "+v"(r0[1]), "+v"(r0[2]), "+v"(r1[0]), "+v"(r1[1]), "+v"(r1[2]), "+v"(r2[0]), "+v"(r2[1]), "+v"(r2[2])::"memory");
    f32x4 v[5];
#pragma unroll
    for (int i = 0; i < 5; ++i) v[i] = acc[i][0];
#pragma unroll
    for (int n = 0; n < 3; ++n) {
      v[0] += f32x4{bflo(r0[n][0]), bfhi(r0[n][0]), bflo(r0[n][1]), bfhi(r0[n][1])};
      v[1] += f32x4{bflo(r0[n][2]), bfhi(r0[n][2]), bflo(r0[n][3]), bfhi(r0[n][3])};
      v[2] += f32x4{bflo(r1[n][0]), bfhi(r1[n][0]), bflo(r1[n][1]), bfhi(r1[n][1])};
      v[3] += f32x4{bflo(r1[n][2]), bfhi(r1[n][2]), bflo(r1[n][3]), bfhi(r1[n][3])};
      v[4] += f32x4{bflo(r2[n][0]), bfhi(r2[n][0]), bflo(r2[n][1]), bfhi(r2[n][1])};
    }
    const int t = q0 + wq * 64 + var * 16 + fr;
    u32x4 o0 = {res(h0[0], v[0][0], v[0][1]), res(h0[1], v[0][2], v[0][3]), res(h0[2], v[1][0], v[1][1]), res(h0[3], v[1][2], v[1][3])};
    u32x4 o1 = {res(h1[0], v[2][0], v[2][1]), res(h1[1], v[2][2], v[2][3]), res(h1[2], v[3][0], v[3][1]), res(h1[3], v[3][2], v[3][3])};
    u32x2 o2 = {res(h2[0], v[4][0], v[4][1]), res(h2[1], v[4][2], v[4][3])};
    char* hb = (char*)a.h;
    *(u32x4*)(hb + toff(t, p0 + wp * 64 + fq * 8, D_)) = o0;
    *(u32x4*)(hb + toff(t, p0 + wp * 64 + 32 + fq * 8, D_)) = o1;
    *(u32x2*)(hb + toff(t, p0 + 128 + wp * 16 + fq * 4, D_)) = o2;
    float s = 0.f, q = 0.f;
    auto accum = [&](u32 w) {
      const bf16x2_t b = __builtin_bit_cast(bf16x2_t, w), ones = __builtin_bit_cast(bf16x2_t, 0x3f803f80u);
      s = __builtin_amdgcn_fdot2_f32_bf16(b, ones, s, false);
      q = __builtin_amdgcn_fdot2_f32_bf16(b, b, q, false);
    };
#pragma unroll
    for (int k = 0; k < 4; ++k) { accum(o0[k]); accum(o1[k]); }
    accum(o2[0]); accum(o2[1]);
    s += __shfl_xor(s, 16); q += __shfl_xor(q, 16);
    s += __shfl_xor(s, 32); q += __shfl_xor(q, 32);
    if (fq == 0) *(f32x2*)(a.hstat + (size_t)t * 16 + (eb * 2 + wp) * 2) = f32x2{s, q};
  } else if (MODE == 4) {
    f32x4 fin[PT];
    char* xw = smem + (wave * PT) * 1024 + lane * 16;
    const char* xr = smem + ((wave ^ 2) * PT) * 1024 + lane * 16;
    if (wk == 0) {
#pragma unroll
      for (int i = 0; i < PT; ++i) { *(f32x4*)(xw + i * 1024) = acc[i][1]; fin[i] = acc[i][0]; }
    } else {
#pragma unroll
      for (int i = 0; i < PT; ++i) { *(f32x4*)(xw + i * 1024) = acc[i][0]; fin[i] = acc[i][1]; }
    }
    bar();
    const int t = q0 + wq * 32 + wk * 16 + fr;
    f32x4 v[PT];
#pragma unroll
    for (int i = 0; i < PT; ++i) v[i] = fin[i] + *(const f32x4*)(xr + i * 1024);
    u16* outp = a.out + (size_t)t * D_ + p0;
    u32x4 o0 = {res(h0[0], v[0][0], v[0][1]), res(h0[1], v[0][2], v[0][3]), res(h0[2], v[1][0], v[1][1]), res(h0[3], v[1][2], v[1][3])};
    u32x4 o1 = {res(h1[0], v[2][0], v[2][1]), res(h1[1], v[2][2], v[2][3]), res(h1[2], v[3][0], v[3][1]), res(h1[3], v[3][2], v[3][3])};
    u32x2 o2 = {res(h2[0], v[4][0], v[4][1]), res(h2[1], v[4][2], v[4][3])};
    *(u32x4*)(outp + fq * 8) = o0;
    *(u32x4*)(outp + 32 + fq * 8) = o1;
    *(u32x2*)(outp + 64 + fq * 4) = o2;
  } else {
    bar();
    f32x4 cs[6];
    f32x2 ts[4];
#pragma unroll
    for (int i = 0; i < 6; ++i) cs[i] = *(const f32x4*)(smem + (wp * 96 + i * 16 + fq * 4) * 4);
#pragma unroll
    for (int n = 0; n < 4; ++n) ts[n] = *(const f32x2*)(smem + 1024 + (wq * 64 + n * 16 + fr) * 8);
    char* outp = (char*)a.act;
    constexpr float K1c = -2.f * 0.7978845608028654f * 1.4426950408889634f, K2c = K1c * 0.044715f;
#pragma unroll
    for (int n = 0; n < QT; ++n) {
      const int t = q0 + wq * 64 + n * 16 + fr;
      const float r = ts[n][1], nm = -ts[n][0];
      u32 o[6];
#pragma unroll
      for (int i = 0; i < 3; ++i) {
        f32x4 g = (acc[2 * i][n] + nm * cs[2 * i]) * r;
        f32x4 u = (acc[2 * i + 1][n] + nm * cs[2 * i + 1]) * r;
        f32x4 z = g * (g * g * K2c + K1c);
        f32x4 e = {__builtin_amdgcn_exp2f(z[0]), __builtin_amdgcn_exp2f(z[1]), __builtin_amdgcn_exp2f(z[2]), __builtin_amdgcn_exp2f(z[3])};
        e = e + 1.f;
        f32x4 rc = {__builtin_amdgcn_rcpf(e[0]), __builtin_amdgcn_rcpf(e[1]), __builtin_amdgcn_rcpf(e[2]), __builtin_amdgcn_rcpf(e[3])};
        f32x4 pr = (g * rc) * u;
        o[i * 2] = pack2(pr[0], pr[1]);
        o[i * 2 + 1] = pack2(pr[2], pr[3]);
      }
      *(u32x4*)(outp + toff(t, p0 + wp * 32 + fq * 8, F_)) = u32x4{o[0], o[1], o[2], o[3]};
      *(u32x2*)(outp + toff(t, p0 + 64 + wp * 16 + fq * 4, F_)) = u32x2{o[4], o[5]};
    }
  }
  asm volatile("s_waitcnt vmcnt(0)" : "+v"(csink)::"memory");
}

extern "C" void kernel_launch(void* const* d_in, const int* in_sizes, int n_in, void* d_out, int out_size, void* d_ws,
                              size_t ws_size, hipStream_t stream) {
  Args a;
  a.x = (const u16*)d_in[0];
  a.Cr = (const u16*)d_in[1];
  a.Ci = (const u16*)d_in[2];
  a.Wr = (const u16*)d_in[3];
  a.Wi = (const u16*)d_in[4];
  a.wg = (const u16*)d_in[6];
  a.wu = (const u16*)d_in[7];
  a.wd = (const u16*)d_in[8];
  u16* ws = (u16*)d_ws;
  a.ab = ws;
  a.h = a.ab + 2 * S_ * D_;
  a.wguP = a.h + S_ * D_;
  a.wdP = a.wguP + 2 * F_ * D_;
  a.hstat = (float*)(a.wdP + F_ * D_);
  a.colp = a.hstat + S_ * 16;
  a.flags = (u32*)(a.colp + 10 * 2 * F_);
  a.xbuf = (char*)(a.flags + 1024 * 16);
  a.act = (u16*)(a.xbuf + (size_t)256 * 16 * 2560);
  a.out = (u16*)d_out;
  gemm_kernel<1><<<256, 512, 0, stream>>>(a);
  gemm_kernel<2><<<256, 512, 0, stream>>>(a);
  gemm_kernel<3><<<256, 512, 0, stream>>>(a);
  gemm_kernel<4><<<256, 512, 0, stream>>>(a);
}
```
